```python
import torch

def _softmax(x, axis=-1):
    return torch.softmax(x, dim=axis)
def _gelu(x):
    return torch.nn.functional.gelu(x, approximate="tanh")
def _silu(x):
    return torch.nn.functional.silu(x)
def _sigmoid(x):
    return torch.sigmoid(x)
def _layernorm(x, axis=-1, eps=1e-5):
    m = x.mean(dim=axis, keepdim=True)
    v = x.var(dim=axis, keepdim=True, unbiased=False)
    return (x - m) / torch.sqrt(v + eps)
def _scatter_add(tgt, idx, src, axis=0):
    return tgt.clone().index_add_(axis, idx, src)
def _segment_sum(src, seg, n):
    out = torch.zeros((n,) + src.shape[1:], dtype=src.dtype)
    return out.index_add_(0, seg, src)
def _pad(x, pw):
    flat = tuple(v for pair in reversed(pw) for v in pair)
    return torch.nn.functional.pad(x, flat)

kernel_name = 'standalone__molecular_angular_gemnet__graph'

def setup_inputs():

    _w = (lambda a: (lambda t: t.to(torch.bfloat16) if t.is_floating_point() else t)(torch.as_tensor(a)))
    N, deg, De, L = 10000, 16, 16, 2
    E = N * deg
    T = E * deg
    NR = 8
    dist = torch.rand(E).to(float)
    angle = (torch.rand(T) * torch.pi).to(float)
    centers = torch.linspace(0.0, 1.0, NR).to(float)
    ang_freq = torch.arange(1, L + 1).to(float)
    return {
        "m": _w((lambda *s: torch.randn(*s, dtype=torch.bfloat16) * 0.05)(E, De)),
        "idx_in": _w((lambda lo, hi, *s: torch.randint(lo, hi, s))(0, E, T).to(torch.int64)),
        "idx_out": _w((lambda lo, hi, *s: torch.randint(lo, hi, s))(0, E, T).to(torch.int64)),
        "dist": _w(dist),
        "angle": _w(angle),
        "centers": _w(centers),
        "ang_freq": _w(ang_freq),
        "w_rbf": _w((lambda *s: torch.randn(*s, dtype=torch.bfloat16) * 0.2)(NR, De)),
        "w_sbf": _w((lambda *s: torch.randn(*s, dtype=torch.bfloat16) * 0.2)(NR * L, De)),
        "w_down": _w((lambda *s: torch.randn(*s, dtype=torch.bfloat16) * 0.2)(De, De)),
        "w_up": _w((lambda *s: torch.randn(*s, dtype=torch.bfloat16) * 0.2)(De, De)),
    }

def reference(m, idx_in, idx_out, dist, angle, centers, ang_freq, w_rbf, w_sbf, w_down, w_up):
    E, De = m.shape[0], m.shape[1]
    NR = centers.shape[0]
    L = ang_freq.shape[0]
    rbf = torch.exp(-10.0 * (dist[:, None] - centers[None, :]) ** 2)
    cbf = torch.cos(ang_freq[None, :] * angle[:, None])
    rbf_t = rbf[idx_out]
    sbf = torch.reshape(rbf_t[:, :, None] * cbf[:, None, :], (-1, NR * L))
    x = (m[idx_in] @ w_down) * (sbf @ w_sbf)
    agg = _segment_sum(x, idx_out, E)
    return m + (agg @ w_up) * (rbf @ w_rbf)
```

```cpp
#include <hip/hip_runtime.h>
#include <hip/hip_bf16.h>
#include <stdint.h>

static __device__ __forceinline__ float bf2f(uint16_t x) { return __uint_as_float(((uint32_t)x) << 16); }
static __device__ __forceinline__ uint16_t f2bf(float f) { __hip_bfloat16 h = __float2bfloat16(f); return *reinterpret_cast<uint16_t*>(&h); }
static __device__ __forceinline__ float rbf16(float f) { return bf2f(f2bf(f)); }

__global__ void k_zero(float* p, int n) { int i = blockIdx.x * blockDim.x + threadIdx.x; if (i < n) p[i] = 0.f; }

__global__ void k_scatter(const uint16_t* m, const int* idx_in, const int* idx_out, const uint16_t* angle, const uint16_t* freq, float* U, int T) {
    int g = blockIdx.x * blockDim.x + threadIdx.x;
    int t = g >> 5; int j = g & 31; if (t >= T) return;
    int l = j >> 4, c = j & 15;
    float a = bf2f(angle[t]);
    float cb = rbf16(cosf(rbf16(bf2f(freq[l]) * a)));
    float v = bf2f(m[(size_t)idx_in[t] * 16 + c]);
    atomicAdd(&U[(size_t)idx_out[t] * 32 + j], cb * v);
}

__global__ void k_epi(const uint16_t* m, const uint16_t* dist, const uint16_t* centers, const uint16_t* w_rbf, const uint16_t* w_sbf,
                      const uint16_t* w_down, const uint16_t* w_up, const float* U, uint16_t* out, int E) {
    int e = blockIdx.x * blockDim.x + threadIdx.x; if (e >= E) return;
    float rbf[8];
    float d = bf2f(dist[e]);
    for (int r = 0; r < 8; ++r) { float x = rbf16(d - bf2f(centers[r])); float q = rbf16(x * x); rbf[r] = rbf16(expf(rbf16(-10.f * q))); }
    float agg[16];
    for (int c = 0; c < 16; ++c) {
        float s0 = 0, s1 = 0, a0 = 0, a1 = 0;
        for (int k = 0; k < 16; ++k) { float w = bf2f(w_down[k * 16 + c]); s0 += U[(size_t)e * 32 + k] * w; s1 += U[(size_t)e * 32 + 16 + k] * w; }
        for (int r = 0; r < 8; ++r) { a0 += rbf[r] * bf2f(w_sbf[(r * 2 + 0) * 16 + c]); a1 += rbf[r] * bf2f(w_sbf[(r * 2 + 1) * 16 + c]); }
        agg[c] = a0 * s0 + a1 * s1;
    }
    for (int c = 0; c < 16; ++c) {
        float y = 0, g = 0;
        for (int k = 0; k < 16; ++k) y += agg[k] * bf2f(w_up[k * 16 + c]);
        for (int r = 0; r < 8; ++r) g += rbf[r] * bf2f(w_rbf[r * 16 + c]);
        out[(size_t)e * 16 + c] = f2bf(bf2f(m[(size_t)e * 16 + c]) + y * g);
    }
}

extern "C" void kernel_launch(void* const* d_in, const int* in_sizes, int n_in,
                              void* d_out, int out_size, void* d_ws, size_t ws_size,
                              hipStream_t stream) {
    const uint16_t* m = (const uint16_t*)d_in[0];
    const int* idx_in = (const int*)d_in[1];
    const int* idx_out = (const int*)d_in[2];
    const uint16_t* dist = (const uint16_t*)d_in[3];
    const uint16_t* angle = (const uint16_t*)d_in[4];
    const uint16_t* centers = (const uint16_t*)d_in[5];
    const uint16_t* freq = (const uint16_t*)d_in[6];
    const uint16_t* w_rbf = (const uint16_t*)d_in[7];
    const uint16_t* w_sbf = (const uint16_t*)d_in[8];
    const uint16_t* w_down = (const uint16_t*)d_in[9];
    const uint16_t* w_up = (const uint16_t*)d_in[10];
    int E = in_sizes[3]; int T = in_sizes[1];
    float* U = (float*)d_ws;
    k_zero<<<(E * 32 + 255) / 256, 256, 0, stream>>>(U, E * 32);
    k_scatter<<<(int)(((size_t)T * 32 + 255) / 256), 256, 0, stream>>>(m, idx_in, idx_out, angle, freq, U, T);
    k_epi<<<(E + 255) / 256, 256, 0, stream>>>(m, dist, centers, w_rbf, w_sbf, w_down, w_up, U, (uint16_t*)d_out, E);
}
```

```cpp
#include <hip/hip_runtime.h>
#include <hip/hip_bf16.h>
#include <stdint.h>

typedef short bf16x8 __attribute__((ext_vector_type(8)));
typedef short bf16x4 __attribute__((ext_vector_type(4)));
typedef float f32x4 __attribute__((ext_vector_type(4)));
typedef float f32x2 __attribute__((ext_vector_type(2)));
typedef __bf16 bf2_t __attribute__((ext_vector_type(2)));
typedef uint32_t u32x4 __attribute__((ext_vector_type(4)));
typedef uint32_t u32x2 __attribute__((ext_vector_type(2)));
typedef uint16_t __attribute__((may_alias)) u16a;
typedef uint32_t __attribute__((may_alias)) u32a;

#define DI __device__ __forceinline__
#define STAMP(K, i)
#define WSTAMP(i)

constexpr int E_ = 160000, T_ = 2560000;
constexpr int NB = 250;
constexpr int EB = 640;
constexpr int NTILE = 40;
constexpr int CH = 10240;
constexpr int TABW = 256;
constexpr uint32_t HALF = 80000;
constexpr uint32_t IDXMASK = 0x3FFFFu;
constexpr uint32_t INV = 0xFFFFFFFFu;

DI float bf2f(uint32_t x) { return __uint_as_float(x << 16); }
DI uint32_t cvt_pk(float lo, float hi) { f32x2 v = {lo, hi}; bf2_t r = __builtin_convertvector(v, bf2_t); return __builtin_bit_cast(uint32_t, r); }
DI float rbf16(float f) { return __uint_as_float(cvt_pk(f, 0.f) << 16); }
DI float cos_rad(float x) { return __builtin_amdgcn_cosf(__builtin_amdgcn_fractf(x * 0.15915494309189535f)); }
DI uint32_t mulhi24(uint32_t a, uint32_t b) { uint32_t r; asm("v_mul_hi_u32_u24 %0, %1, %2" : "=v"(r) : "v"(a), "v"(b)); return r; }
DI uint32_t row_scan(uint32_t x) {
    x += __builtin_amdgcn_update_dpp(0u, x, 0x111, 0xf, 0xf, false);
    x += __builtin_amdgcn_update_dpp(0u, x, 0x112, 0xf, 0xf, false);
    x += __builtin_amdgcn_update_dpp(0u, x, 0x114, 0xf, 0xf, false);
    x += __builtin_amdgcn_update_dpp(0u, x, 0x118, 0xf, 0xf, false);
    return x;
}
DI uint32_t wave_scan(uint32_t x) {
    x = row_scan(x);
    x += __builtin_amdgcn_update_dpp(0u, x, 0x142, 0xa, 0xf, false);
    x += __builtin_amdgcn_update_dpp(0u, x, 0x143, 0xc, 0xf, false);
    return x;
}

#define PF0_MODE 0
#define RECAUX 16
#define PF1_AT 3

typedef short s16x4 __attribute__((ext_vector_type(4)));
typedef uint32_t u32x3 __attribute__((ext_vector_type(3)));
typedef __attribute__((address_space(3))) s16x4* lds_s16x4_p;
typedef __attribute__((address_space(1))) unsigned long long gu64;
typedef __attribute__((address_space(3))) volatile uint32_t lds_vu32;

template <int I> DI uint32_t row_bcast(uint32_t x) { return (uint32_t)__builtin_amdgcn_update_dpp(0, (int)x, 0x150 + I, 0xf, 0xf, false); }
DI uint32_t row_bcast_i(uint32_t x, int i) {
    switch (i) { case 0: return row_bcast<0>(x); case 1: return row_bcast<1>(x); case 2: return row_bcast<2>(x); case 3: return row_bcast<3>(x);
                 case 4: return row_bcast<4>(x); case 5: return row_bcast<5>(x); case 6: return row_bcast<6>(x); default: return row_bcast<7>(x); }
}
constexpr int CHB = CH * 6;
constexpr int B_HIST = CHB;
constexpr int B_BASE = B_HIST + 1024;
constexpr int CAPT = 192;
constexpr int LSTR = (CAPT + 1) * 8;
constexpr int A_HIST = 0;
constexpr int A_FLAG = 384;
constexpr int A_NULL = 512;
constexpr int A_LIST = 768;
constexpr int A_STAGE = A_LIST + 80 * LSTR;
constexpr int A_TRASH = A_STAGE + 8 * 4096;
constexpr int A_WFR = A_TRASH + 1024;
constexpr int LDSF = A_WFR + 5 * 512 + 1024;
static_assert(A_STAGE % 16 == 0 && B_BASE + 1024 <= A_TRASH && LDSF <= 163840, "lds map");
constexpr size_t WS_EP = 0;
constexpr size_t WS_TAB = 4096;
constexpr size_t WS_REC = WS_TAB + (size_t)NB * TABW * 8;
constexpr size_t WS_ACC = WS_REC + (size_t)NB * CHB;
constexpr unsigned long long TAGMASK = (1ull << 36) - 1;

DI void dma16(uint32_t m0val, uint32_t voff, const __amdgpu_buffer_rsrc_t rs) {
    asm volatile("s_mov_b32 m0, %0\n\ts_nop 0\n\tbuffer_load_dwordx4 %1, %2, 0 offen lds" :: "s"(m0val), "v"(voff), "s"(rs) : "memory");
}
DI void dma16_dep(uint32_t m0val, uint32_t voff, const __amdgpu_buffer_rsrc_t rs, s16x4 d0, s16x4 d1) {
    asm volatile("s_mov_b32 m0, %0\n\ts_nop 0\n\tbuffer_load_dwordx4 %1, %2, 0 offen lds" :: "s"(m0val), "v"(voff), "s"(rs), "v"(d0), "v"(d1) : "memory");
}

__global__ __launch_bounds__(1024) void k_fused(const uint16_t* __restrict__ m, const int* __restrict__ idx_in, const int* __restrict__ idx_out,
                                                const uint16_t* __restrict__ dist, const uint16_t* __restrict__ angle, const uint16_t* __restrict__ centers,
                                                const uint16_t* __restrict__ freq, const uint16_t* __restrict__ w_rbf, const uint16_t* __restrict__ w_sbf,
                                                const uint16_t* __restrict__ w_down, const uint16_t* __restrict__ w_up, char* __restrict__ ws,
                                                uint16_t* __restrict__ out) {
    extern __shared__ __attribute__((aligned(16))) char smem[];
    const int tid = threadIdx.x, lane = tid & 63;
    const int v = __builtin_amdgcn_readfirstlane(tid >> 6);
    const int w = blockIdx.x;
    const unsigned long long tag = (*(const unsigned long long*)(ws + WS_EP) + 1ull) & TAGMASK;
    const int bx = blockIdx.x, xc = bx & 7, q = bx >> 3;
    const int c = (xc < 2 ? xc * 32 : 64 + (xc - 2) * 31) + q;
    STAMP(0, 0);
    {
        uint32_t* hist = (uint32_t*)(smem + B_HIST);
        uint32_t* basearr = (uint32_t*)(smem + B_BASE);
        if (tid < 256) hist[tid] = 0;
        const int* po = idx_out + (size_t)w * CH; const int* pi = idx_in + (size_t)w * CH; const uint16_t* pa = angle + (size_t)w * CH;
        const u32x4 o0 = ((const u32x4*)po)[tid], o1 = ((const u32x4*)po)[1024 + tid]; const u32x2 o2 = ((const u32x2*)(po + 8192))[tid];
        asm volatile("s_waitcnt lgkmcnt(0)\n\ts_barrier" ::: "memory");
        const u32x4 i0 = ((const u32x4*)pi)[tid]; const u32x2 a0 = ((const u32x2*)pa)[tid];
        const u32x4 i1 = ((const u32x4*)pi)[1024 + tid]; const u32x2 a1 = ((const u32x2*)pa)[1024 + tid];
        const u32x2 i2 = ((const u32x2*)(pi + 8192))[tid]; const uint32_t a2 = ((const uint32_t*)(pa + 8192))[tid];
        const uint32_t o[10] = {o0[0], o0[1], o0[2], o0[3], o1[0], o1[1], o1[2], o1[3], o2[0], o2[1]};
        const uint32_t in[10] = {i0[0], i0[1], i0[2], i0[3], i1[0], i1[1], i1[2], i1[3], i2[0], i2[1]};
        const uint32_t ap[5] = {a0[0], a0[1], a1[0], a1[1], a2};
        uint32_t br[10], code[10];
#pragma unroll
        for (int j = 0; j < 10; ++j) {
            const uint32_t b = mulhi24(o[j], 6710887u);
            const uint32_t el = o[j] - b * EB;
            br[j] = (b << 18) | atomicAdd(&hist[b], 1u);
            code[j] = (el << 20) | (((0u - (el >> 2)) & 3u) << 18);
        }
        STAMP(0, 2);
        __syncthreads();
        STAMP(0, 3);
        const u32x4 c4 = ((const u32x4*)hist)[lane];
        const uint32_t s4 = c4[0] + c4[1] + c4[2] + c4[3];
        const uint32_t ex = wave_scan(s4) - s4;
        const u32x4 b4 = {ex, ex + c4[0], ex + c4[0] + c4[1], ex + c4[0] + c4[1] + c4[2]};
        ((u32x4*)basearr)[lane] = b4;
#pragma unroll
        for (int j = 0; j < 10; ++j) {
            const uint32_t pos = *(const u32a*)(smem + B_BASE + (br[j] >> 16)) + (br[j] & 0xFFFFu);
            const uint32_t pb = (pos >> 1) * 12, od = pos & 1;
            *(u32a*)(smem + pb + od * 4) = (in[j] | code[j]) + (in[j] >= HALF ? (uint32_t)NTILE << 24 : 0u);
            *(u16a*)(smem + pb + 8 + od * 2) = (uint16_t)((j & 1) ? (ap[j >> 1] >> 16) : ap[j >> 1]);
        }
        STAMP(0, 4);
        __syncthreads();
        STAMP(0, 5);
        {
            const auto rs = __builtin_amdgcn_make_buffer_rsrc(ws + WS_REC + (size_t)w * CHB, 0, CHB, 0x00020000);
#pragma unroll
            for (int k = 0; k < 4; ++k) {
                const int off = (k * 1024 + tid) * 16;
                if (k < 3 || tid < 768) __builtin_amdgcn_raw_buffer_store_b128(*(const u32x4*)(smem + off), rs, off, 0, 16);
            }
        }
        STAMP(0, 6);
        asm volatile("s_waitcnt vmcnt(0)" ::: "memory");
        STAMP(0, 7);
        __syncthreads();
        if (v == 0) {
            const auto rs = __builtin_amdgcn_make_buffer_rsrc(ws + WS_TAB + (size_t)w * TABW * 8, 0, TABW * 8, 0x00020000);
            const uint32_t thi = (uint32_t)(tag >> 4), tlo = (uint32_t)(tag << 28);
            const u32x4 g0 = {tlo | (c4[0] << 14) | b4[0], thi, tlo | (c4[1] << 14) | b4[1], thi};
            const u32x4 g1 = {tlo | (c4[2] << 14) | b4[2], thi, tlo | (c4[3] << 14) | b4[3], thi};
            __builtin_amdgcn_raw_buffer_store_b128(g0, rs, lane * 32, 0, 16);
            __builtin_amdgcn_raw_buffer_store_b128(g1, rs, lane * 32 + 16, 0, 16);
        }
        STAMP(0, 8);
        WSTAMP(4);
    }
    uint32_t* hist = (uint32_t*)(smem + A_HIST);
    uint32_t* flag = (uint32_t*)(smem + A_FLAG);
    char* stage = smem + A_STAGE + (v & 7) * 4096;
    const uint32_t ring = A_STAGE + (v & 7) * 4096 + 2048;
    const int x = lane & 15, g = lane >> 4;
    if (tid < 96) hist[tid] = 0;
    if (tid < 32) *(u32x2*)(smem + A_NULL + tid * 8) = (u32x2){IDXMASK, 0u};
    if (tid < 2) flag[tid] = 0;
    if (v < 8) {
        *(u32x4*)(stage + lane * 16) = (u32x4){0u, 0u, 0u, 0u};
        *(u32x4*)(stage + 1024 + lane * 16) = (u32x4){0u, 0u, 0u, 0u};
    }
    const uint32_t fq = *(const uint32_t*)freq;
    const float f0 = bf2f(fq & 0xFFFFu), f1 = __uint_as_float(fq & 0xFFFF0000u);
    __syncthreads();
    uint32_t dv[5]; u32x2 mrow[5];
#pragma unroll
    for (int s = 0; s < 5; ++s) {
        const int e = c * EB + ((v & 7) + 8 * s) * 16 + x;
        dv[s] = dist[e];
        mrow[s] = *(const u32x2*)(m + (size_t)e * 16 + 4 * g);
    }
    if (v == 15) {
        bf16x4 Asbf0, Asbf1, Arbf, Aup, Adn; f32x4 cen;
#pragma unroll
        for (int j = 0; j < 4; ++j) {
            const int r = (4 * g + j) & 7;
            Asbf0[j] = (short)w_sbf[(r * 2 + 0) * 16 + x];
            Asbf1[j] = (short)w_sbf[(r * 2 + 1) * 16 + x];
            Arbf[j] = (short)w_rbf[r * 16 + x];
            Aup[j] = (short)w_up[(4 * g + j) * 16 + x];
            Adn[j] = (short)w_down[(4 * g + j) * 16 + x];
            cen[j] = bf2f(centers[r]);
        }
        *(bf16x4*)(smem + A_WFR + 0 * 512 + lane * 8) = Asbf0;
        *(bf16x4*)(smem + A_WFR + 1 * 512 + lane * 8) = Asbf1;
        *(bf16x4*)(smem + A_WFR + 2 * 512 + lane * 8) = Arbf;
        *(bf16x4*)(smem + A_WFR + 3 * 512 + lane * 8) = Aup;
        *(bf16x4*)(smem + A_WFR + 4 * 512 + lane * 8) = Adn;
        *(f32x4*)(smem + A_WFR + 5 * 512 + lane * 16) = cen;
    }
    const int wseg = v + 16 * (2 * (x & 7) + (g >> 1));
    uint32_t s_start = 0, s_end = 0;
    {
        const gu64* gp = (const gu64*)(ws + WS_TAB) + (size_t)min(wseg, NB - 1) * TABW + c;
        unsigned long long gr;
        for (unsigned spins = 0;; ++spins) {
            gr = __hip_atomic_load(gp, __ATOMIC_RELAXED, __HIP_MEMORY_SCOPE_AGENT);
            if (__builtin_amdgcn_ballot_w64((gr >> 28) != tag) == 0) break;
            if (spins > (1u << 18)) { gr = 0; break; }
            __builtin_amdgcn_s_sleep(2);
        }
        if (wseg < NB) { s_start = wseg * CH + ((uint32_t)gr & 0x3FFFu); s_end = s_start + (((uint32_t)gr >> 14) & 0x3FFFu); }
    }
    STAMP(1, 0);
    WSTAMP(0);
    const auto rsrc_m = __builtin_amdgcn_make_buffer_rsrc((void*)m, 0, E_ * 32, 0x00020000);
    const auto rsrc_r = __builtin_amdgcn_make_buffer_rsrc(ws + WS_REC, 0, NB * CHB, 0x00020000);
    const uint32_t s_p0 = s_start >> 1;
    const uint32_t s_rn = s_end > s_start ? s_end - 2 * s_p0 : 0u;
    const uint32_t t2 = 2 * (lane & 31);

    uint32_t dr = 0;
    uint32_t pend = 0;
    bool wave_pending = false;
    for (bool first = true;; first = false) {
        const bool fresh = !wave_pending;
        if (!fresh) dr -= 80;
        const bool anylong = __builtin_amdgcn_ballot_w64(s_rn > dr + 64) != 0;
        const uint32_t left = s_rn > dr ? min(s_rn - dr, 127u) : 0u;
        const uint32_t wd = left == 0 ? 0u : ((s_p0 + (dr >> 1)) * 12) | (left << 24) | ((dr == 0 ? s_start & 1u : 0u) << 31);
        u32x3 pr[10]; uint32_t dsc[10];
#pragma unroll
        for (int i = 0; i < 8; ++i) {
            dsc[i] = row_bcast_i(wd, i);
            uint32_t off = (dsc[i] & 0xFFFFFFu) + t2 * 6;
            off = t2 < ((dsc[i] >> 24) & 127u) ? off : 0x7FFFFFF0u;
            asm volatile("" : "+v"(off));
            pr[i] = __builtin_amdgcn_raw_buffer_load_b96(rsrc_r, off, 0, RECAUX);
        }
        if (anylong) {
#pragma unroll
            for (int i = 8; i < 10; ++i) {
                const int sg = 8 * (i - 8) + (lane >> 3);
                dsc[i] = (uint32_t)__builtin_amdgcn_ds_bpermute(((sg >> 1) + 32 * (sg & 1)) * 4, (int)wd);
                const uint32_t tl = 64 + 2 * (lane & 7);
                uint32_t off = (dsc[i] & 0xFFFFFFu) + tl * 6;
                off = tl < ((dsc[i] >> 24) & 127u) ? off : 0x7FFFFFF0u;
                asm volatile("" : "+v"(off));
                pr[i] = __builtin_amdgcn_raw_buffer_load_b96(rsrc_r, off, 0, RECAUX);
            }
        }
        dr += 80;
        if (first) { STAMP(1, 1); WSTAMP(1); }
        uint32_t newpend = 0;
#pragma unroll
        for (int i0 = 0; i0 < 10; i0 += 2) {
            if (i0 < 8 || anylong) {
                uint64_t mk[4]; uint32_t ha[4];
#pragma unroll
                for (int t = 0; t < 4; ++t) {
                    const int i = i0 + (t >> 1), r = 2 * i + (t & 1);
                    const uint32_t tt = (i < 8 ? t2 : 64 + 2 * (lane & 7)) + (t & 1), n = (dsc[i] >> 24) & 127u;
                    bool ok = tt < n;
                    if ((t & 1) == 0 && i < 8) ok = ok && !(t2 == 0 && (int)dsc[i] < 0);
                    mk[t] = __builtin_amdgcn_ballot_w64(ok);
                    ha[t] = (pr[i][t & 1] >> 24) * 4;
                    (void)r;
                }
                if (__builtin_expect(!fresh, 0)) {
#pragma unroll
                    for (int t = 0; t < 4; ++t) mk[t] &= __builtin_amdgcn_ballot_w64(((pend >> (2 * i0 + t)) & 1u) != 0);
                }
                uint32_t rank[4] = {0u, 0u, 0u, 0u};
                const uint32_t one = 1u;
                asm volatile("s_mov_b64 exec, %4\n\tds_add_rtn_u32 %0, %8, %12 offset:%c13\n\t"
                             "s_mov_b64 exec, %5\n\tds_add_rtn_u32 %1, %9, %12 offset:%c13\n\t"
                             "s_mov_b64 exec, %6\n\tds_add_rtn_u32 %2, %10, %12 offset:%c13\n\t"
                             "s_mov_b64 exec, %7\n\tds_add_rtn_u32 %3, %11, %12 offset:%c13\n\t"
                             "s_mov_b64 exec, -1"
                             : "+v"(rank[0]), "+v"(rank[1]), "+v"(rank[2]), "+v"(rank[3])
                             : "s"(mk[0]), "s"(mk[1]), "s"(mk[2]), "s"(mk[3]), "v"(ha[0]), "v"(ha[1]), "v"(ha[2]), "v"(ha[3]), "v"(one), "i"(A_HIST)
                             : "memory");
                u32x2 o[4]; uint32_t lb[4];
#pragma unroll
                for (int t = 0; t < 4; ++t) {
                    const int i = i0 + (t >> 1);
                    const float a = (t & 1) ? __uint_as_float(pr[i][2] & 0xFFFF0000u) : __uint_as_float(pr[i][2] << 16);
                    const uint32_t pp = cvt_pk(f0 * a, f1 * a);
                    o[t][0] = pr[i][t & 1];
                    o[t][1] = cvt_pk(cos_rad(__uint_as_float(pp << 16)), cos_rad(__uint_as_float(pp & 0xFFFF0000u)));
                    lb[t] = (o[t][0] >> 24) * LSTR;
                }
                asm volatile("s_waitcnt lgkmcnt(0)" : "+v"(rank[0]), "+v"(rank[1]), "+v"(rank[2]), "+v"(rank[3]), "+v"(o[0][1]), "+v"(o[1][1]), "+v"(o[2][1]), "+v"(o[3][1]) :: "memory");
                uint32_t wa[4];
#pragma unroll
                for (int t = 0; t < 4; ++t) wa[t] = lb[t] + min(rank[t], (uint32_t)CAPT) * 8;
                asm volatile("s_mov_b64 exec, %0\n\tds_write_b64 %4, %8 offset:%c12\n\t"
                             "s_mov_b64 exec, %1\n\tds_write_b64 %5, %9 offset:%c12\n\t"
                             "s_mov_b64 exec, %2\n\tds_write_b64 %6, %10 offset:%c12\n\t"
                             "s_mov_b64 exec, %3\n\tds_write_b64 %7, %11 offset:%c12\n\t"
                             "s_mov_b64 exec, -1"
                             :: "s"(mk[0]), "s"(mk[1]), "s"(mk[2]), "s"(mk[3]), "v"(wa[0]), "v"(wa[1]), "v"(wa[2]), "v"(wa[3]), "v"(o[0]), "v"(o[1]), "v"(o[2]), "v"(o[3]), "i"(A_LIST)
                             : "memory");
                if (__builtin_expect(__builtin_amdgcn_ballot_w64(max(max(rank[0], rank[1]), max(rank[2], rank[3])) >= CAPT) != 0, 0)) {
#pragma unroll
                    for (int t = 0; t < 4; ++t) newpend |= (rank[t] >= CAPT ? 1u : 0u) << (2 * i0 + t);
                }
            }
        }
        pend = newpend;
        wave_pending = __builtin_amdgcn_ballot_w64(pend != 0) != 0;
        const bool wave_more = wave_pending || __builtin_amdgcn_ballot_w64(s_rn > dr) != 0;
        if (wave_more && lane == 0) flag[0] = 1;
        if (first) { STAMP(1, 3); WSTAMP(3); }
        __syncthreads();
        if (first) { STAMP(1, 4); }
        const bool more = flag[0] != 0;
        if (v < 8) {
            int ln = lane; asm volatile("" : "+v"(ln));
            const int xx = ln & 15, gg = ln >> 4;
            const int rho = ln >> 1;
            const int kpos = 8 * ((rho >> 2) & 3) + 4 * (rho >> 4) + (rho & 3);
            const uint32_t lsel = ln & 1;
            const uint32_t sc_const = (uint32_t)(((kpos >> 3) * 16) | ((kpos & 7) * 2)) | (lsel * (1024 + 64));
            const uint32_t rd0 = (uint32_t)(xx * 64 + ((gg ^ ((0 - (xx >> 2)) & 3)) * 16));
            const uint32_t rd1 = (uint32_t)(1024 + (xx ^ 1) * 64 + ((gg ^ ((0 - (xx >> 2)) & 3)) * 16));
            const uint32_t vsh = lsel * 16, half16 = lsel * 16, posb = kpos * 8;
            const uint32_t trb = ring + (4 * gg + (xx >> 2)) * 32 + (xx & 3) * 8;
            const int jl = min(ln, 9);
            const int keyl = (jl >= 5 ? NTILE - 40 : 0) + v + 8 * jl;
            const int cntl = min((int)hist[keyl], CAPT);
            const int nsl = ln < 10 ? (cntl + 31) >> 5 : 0;
#pragma unroll
            for (int t = 0; t < 5; ++t) {
                const uint32_t d = (uint32_t)__builtin_amdgcn_ds_bpermute((2 * t + (ln >> 5)) * 4, (int)((uint32_t)(A_LIST + keyl * LSTR) | ((uint32_t)cntl << 20)));
                const uint32_t cnt = d >> 20, pos = cnt + (ln & 31);
                if (pos < ((cnt + 31) & ~31u)) *(u32x2*)(smem + (d & 0xFFFFFu) + pos * 8) = (u32x2){IDXMASK, 0u};
            }
            int nst[10]; int cum = 0; uint32_t stepaddr = A_NULL;
#pragma unroll
            for (int j = 0; j < 10; ++j) {
                const int ns = __builtin_amdgcn_readlane(nsl, j);
                const int key = (j >= 5 ? NTILE - 40 : 0) + v + 8 * j;
                if ((uint32_t)(ln - cum) < (uint32_t)ns) stepaddr = A_LIST + key * LSTR + (ln - cum) * 256;
                nst[j] = ns; cum += ns;
            }
            f32x4 acc0[5], acc1[5];
            const auto rs_park = __builtin_amdgcn_make_buffer_rsrc(ws + WS_ACC + (size_t)(w * 8 + v) * 40 * 256, 0, 40 * 256, 0x00020000);
#pragma unroll
            for (int s = 0; s < 5; ++s) {
#pragma unroll
                for (int j = 0; j < 4; ++j) { acc0[s][j] = 0.f; acc1[s][j] = 0.f; }
            }
            if (!first) {
#pragma unroll
                for (int s = 0; s < 5; ++s) {
#pragma unroll
                    for (int j = 0; j < 4; ++j) {
                        acc0[s][j] = __uint_as_float(__builtin_amdgcn_raw_buffer_load_b32(rs_park, ln * 4, (s * 8 + j) * 256, 0));
                        acc1[s][j] = __uint_as_float(__builtin_amdgcn_raw_buffer_load_b32(rs_park, ln * 4, (s * 8 + 4 + j) * 256, 0));
                    }
                }
                asm volatile("s_waitcnt vmcnt(0)" ::: "memory");
            }
            asm volatile("" : "+v"(dv[0]), "+v"(dv[1]), "+v"(dv[2]), "+v"(dv[3]), "+v"(dv[4]), "+v"(mrow[0]), "+v"(mrow[1]), "+v"(mrow[2]), "+v"(mrow[3]), "+v"(mrow[4]));
            u32x2 r0 = *(const u32x2*)(smem + __builtin_amdgcn_readlane(stepaddr, 0) + posb);
            u32x2 r1 = *(const u32x2*)(smem + __builtin_amdgcn_readlane(stepaddr, 1) + posb);
            dma16(ring, (r0[0] & IDXMASK) * 32 + half16, rsrc_m);
            dma16(ring + 1024, (r1[0] & IDXMASK) * 32 + half16, rsrc_m);
            int i = 0;
            STAMP(1, 5);
#pragma unroll
            for (int j = 0; j < 10; ++j) {
                const int s = j % 5;
                if (j == PF1_AT && v == 0 && lane == 0) *(lds_vu32*)(uintptr_t)(A_FLAG + 4) = 1;
                if (j == 5) { STAMP(1, 6); }
                for (int k = 0; k < nst[j]; ++k) {
                    const uint32_t slot = (i & 1) * 1024;
                    const u32x2 r2 = *(const u32x2*)(smem + __builtin_amdgcn_readlane(stepaddr, i + 2) + posb);
                    const uint32_t sa = ((r0[0] >> 14) & 0x3F0u) ^ sc_const;
                    *(u16a*)(stage + sa) = (uint16_t)(r0[1] >> vsh);
                    asm volatile("s_waitcnt vmcnt(1)" ::: "memory");
                    const s16x4 alo = __builtin_amdgcn_ds_read_tr16_b64_v4i16((lds_s16x4_p)(uintptr_t)(trb + slot));
                    const s16x4 ahi = __builtin_amdgcn_ds_read_tr16_b64_v4i16((lds_s16x4_p)(uintptr_t)(trb + slot + 512));
                    const u32x4 s0 = *(const u32x4*)(stage + rd0);
                    const u32x4 s1 = *(const u32x4*)(stage + rd1);
                    *(u16a*)(stage + sa) = 0;
                    dma16_dep(ring + slot, (r2[0] & IDXMASK) * 32 + half16, rsrc_m, alo, ahi);
                    const bf16x8 a = __builtin_shufflevector(alo, ahi, 0, 1, 2, 3, 4, 5, 6, 7);
                    acc0[s] = __builtin_amdgcn_mfma_f32_16x16x32_bf16(a, __builtin_bit_cast(bf16x8, s0), acc0[s], 0, 0, 0);
                    acc1[s] = __builtin_amdgcn_mfma_f32_16x16x32_bf16(a, __builtin_bit_cast(bf16x8, s1), acc1[s], 0, 0, 0);
                    r0 = r1; r1 = r2; ++i;
                }
                if (j >= 5 && !more) {
                    const int e = c * EB + (v + 8 * s) * 16 + xx;
                    const float d = bf2f(dv[s]);
                    const bf16x4 Asbf0 = *(const bf16x4*)(smem + A_WFR + 0 * 512 + ln * 8), Asbf1 = *(const bf16x4*)(smem + A_WFR + 1 * 512 + ln * 8);
                    const bf16x4 Arbf = *(const bf16x4*)(smem + A_WFR + 2 * 512 + ln * 8), Aup = *(const bf16x4*)(smem + A_WFR + 3 * 512 + ln * 8);
                    const bf16x4 Adn = *(const bf16x4*)(smem + A_WFR + 4 * 512 + ln * 8);
                    const f32x4 cen = *(const f32x4*)(smem + A_WFR + 5 * 512 + ln * 16);
                    float rb[4];
#pragma unroll
                    for (int t = 0; t < 4; ++t) {
                        const float df = rbf16(d - cen[t]);
                        const float sq = rbf16(df * df);
                        const float ar = rbf16(-10.f * sq);
                        rb[t] = gg < 2 ? __expf(ar) : 0.f;
                    }
                    const u32x2 rbp = {cvt_pk(rb[0], rb[1]), cvt_pk(rb[2], rb[3])};
                    const bf16x4 Brbf = __builtin_bit_cast(bf16x4, rbp);
                    const f32x4 z = {0.f, 0.f, 0.f, 0.f};
                    const f32x4 A0 = __builtin_amdgcn_mfma_f32_16x16x16bf16_1k(Asbf0, Brbf, z, 0, 0, 0);
                    const f32x4 A1 = __builtin_amdgcn_mfma_f32_16x16x16bf16_1k(Asbf1, Brbf, z, 0, 0, 0);
                    const f32x4 G = __builtin_amdgcn_mfma_f32_16x16x16bf16_1k(Arbf, Brbf, z, 0, 0, 0);
                    const u32x2 u0 = {cvt_pk(acc0[s][0], acc0[s][1]), cvt_pk(acc0[s][2], acc0[s][3])};
                    const u32x2 u1 = {cvt_pk(acc1[s][0], acc1[s][1]), cvt_pk(acc1[s][2], acc1[s][3])};
                    const f32x4 S0 = __builtin_amdgcn_mfma_f32_16x16x16bf16_1k(Adn, __builtin_bit_cast(bf16x4, u0), z, 0, 0, 0);
                    const f32x4 S1 = __builtin_amdgcn_mfma_f32_16x16x16bf16_1k(Adn, __builtin_bit_cast(bf16x4, u1), z, 0, 0, 0);
                    float ag[4];
#pragma unroll
                    for (int t = 0; t < 4; ++t) ag[t] = A0[t] * S0[t] + A1[t] * S1[t];
                    const u32x2 agp = {cvt_pk(ag[0], ag[1]), cvt_pk(ag[2], ag[3])};
                    const f32x4 Y = __builtin_amdgcn_mfma_f32_16x16x16bf16_1k(Aup, __builtin_bit_cast(bf16x4, agp), z, 0, 0, 0);
                    const float o0 = bf2f(mrow[s][0] & 0xFFFFu) + Y[0] * G[0], o1 = __uint_as_float(mrow[s][0] & 0xFFFF0000u) + Y[1] * G[1];
                    const float o2 = bf2f(mrow[s][1] & 0xFFFFu) + Y[2] * G[2], o3 = __uint_as_float(mrow[s][1] & 0xFFFF0000u) + Y[3] * G[3];
                    const u32x2 ov = {cvt_pk(o0, o1), cvt_pk(o2, o3)};
                    *(u32x2*)(out + (size_t)e * 16 + 4 * gg) = ov;
                }
            }
            if (more) {
#pragma unroll
                for (int s = 0; s < 5; ++s) {
#pragma unroll
                    for (int j = 0; j < 4; ++j) {
                        __builtin_amdgcn_raw_buffer_store_b32(__float_as_uint(acc0[s][j]), rs_park, ln * 4, (s * 8 + j) * 256, 0);
                        __builtin_amdgcn_raw_buffer_store_b32(__float_as_uint(acc1[s][j]), rs_park, ln * 4, (s * 8 + 4 + j) * 256, 0);
                    }
                }
            }
            asm volatile("s_waitcnt vmcnt(0)" ::: "memory");
        } else if (first) {
            while (*(lds_vu32*)(uintptr_t)(A_FLAG + 4) == 0) __builtin_amdgcn_s_sleep(8);
            const auto rs = __builtin_amdgcn_make_buffer_rsrc((void*)(m + ((size_t)HALF + q * 2500) * 16), 0, 80000, 0x00020000);
#pragma unroll
            for (int k = 0; k < 10; ++k) dma16(A_TRASH, (uint32_t)((k * 8 + (v - 8)) * 1024 + lane * 16), rs);
            asm volatile("s_waitcnt vmcnt(0)" ::: "memory");
        }
        if (!more) break;
        __syncthreads();
        if (tid < 96) hist[tid] = 0;
        if (tid == 0) flag[0] = 0;
        __syncthreads();
    }
    STAMP(1, 8);
    if (w == 0 && tid == 0) *(unsigned long long*)(ws + WS_EP) = tag;
}

extern "C" void kernel_launch(void* const* d_in, const int* in_sizes, int n_in,
                              void* d_out, int out_size, void* d_ws, size_t ws_size,
                              hipStream_t stream) {
    (void)hipFuncSetAttribute((const void*)k_fused, hipFuncAttributeMaxDynamicSharedMemorySize, LDSF);
    k_fused<<<NB, 1024, LDSF, stream>>>((const uint16_t*)d_in[0], (const int*)d_in[1], (const int*)d_in[2], (const uint16_t*)d_in[3], (const uint16_t*)d_in[4],
                                        (const uint16_t*)d_in[5], (const uint16_t*)d_in[6], (const uint16_t*)d_in[7], (const uint16_t*)d_in[8],
                                        (const uint16_t*)d_in[9], (const uint16_t*)d_in[10], (char*)d_ws, (uint16_t*)d_out);
}
```

```cpp
#include <hip/hip_runtime.h>
#include <hip/hip_bf16.h>
#include <stdint.h>

typedef short bf16x8 __attribute__((ext_vector_type(8)));
typedef short bf16x4 __attribute__((ext_vector_type(4)));
typedef short s16x4 __attribute__((ext_vector_type(4)));
typedef float f32x4 __attribute__((ext_vector_type(4)));
typedef float f32x2 __attribute__((ext_vector_type(2)));
typedef __bf16 bf2_t __attribute__((ext_vector_type(2)));
typedef uint32_t u32x4 __attribute__((ext_vector_type(4)));
typedef uint32_t u32x2 __attribute__((ext_vector_type(2)));
typedef uint16_t __attribute__((may_alias)) u16a;
typedef uint32_t __attribute__((may_alias)) u32a;
typedef __attribute__((address_space(3))) s16x4* lds_s16x4_p;
typedef __attribute__((address_space(1))) unsigned long long gu64;
typedef __attribute__((address_space(3))) volatile uint32_t lds_vu32;

#define DI __device__ __forceinline__

constexpr int AUX_NT = 2, AUX_SC1 = 16;
constexpr int E_ = 160000;
constexpr int NB = 250;
constexpr int EB = 640;
constexpr int NTILE = 40;
constexpr int CH = 10240;
constexpr uint32_t HALF = 80000;
constexpr uint32_t IDXMASK = 0x3FFFFu;
constexpr int CHB = CH * 6;
constexpr int CHS = CHB + 128;
constexpr int TABW = 256;
constexpr size_t WS_EP = 0;
constexpr size_t WS_TAB = 4096;
constexpr size_t WS_REC = WS_TAB + (size_t)NB * TABW * 8;
constexpr size_t WS_ACC = WS_REC + (size_t)NB * CHS;
constexpr size_t WS_SEG = WS_ACC + (size_t)NB * 16 * 24 * 256;
constexpr unsigned long long TAGMASK = (1ull << 36) - 1;
constexpr int RD = 2;
constexpr int CAPT = 192;
constexpr int LW = (CAPT + 1) * 4, LA = (CAPT + 2) * 2;
constexpr int A_HIST = 0;
constexpr int A_FLAG = 384;
constexpr int A_NULL = 512;
constexpr int A_LISTA = 768;
constexpr int A_LISTW = A_LISTA + 80 * LA;
constexpr int WAVEB = 2048 + RD * 1024;
constexpr int A_STAGE = (A_LISTW + 80 * LW + 15) / 16 * 16;
constexpr int A_WFR = A_STAGE + 16 * WAVEB;
constexpr int LDSF = A_WFR + 5 * 512 + 1024;
constexpr int B_DENSE = 768;
constexpr int B_HIST = B_DENSE + CHB;
constexpr int B_BASE = B_HIST + 1024;
static_assert((size_t)NB * CHS < (1u << 24) && A_STAGE % 16 == 0 && B_DENSE % 16 == 0 && B_BASE + 1024 <= A_STAGE && LDSF <= 163840 && A_LISTW + 80 * LW < 131072, "lds map");

DI float bf2f(uint32_t x) { return __uint_as_float(x << 16); }
DI uint32_t cvt_pk(float lo, float hi) { f32x2 v = {lo, hi}; bf2_t r = __builtin_convertvector(v, bf2_t); return __builtin_bit_cast(uint32_t, r); }
DI float rbf16(float f) { return __uint_as_float(cvt_pk(f, 0.f) << 16); }
DI float cos_rad(float x) { return __builtin_amdgcn_cosf(__builtin_amdgcn_fractf(x * 0.15915494309189535f)); }
DI uint32_t mulhi24(uint32_t a, uint32_t b) { uint32_t r; asm("v_mul_hi_u32_u24 %0, %1, %2" : "=v"(r) : "v"(a), "v"(b)); return r; }
DI uint32_t row_scan(uint32_t x) {
    x += __builtin_amdgcn_update_dpp(0u, x, 0x111, 0xf, 0xf, false);
    x += __builtin_amdgcn_update_dpp(0u, x, 0x112, 0xf, 0xf, false);
    x += __builtin_amdgcn_update_dpp(0u, x, 0x114, 0xf, 0xf, false);
    x += __builtin_amdgcn_update_dpp(0u, x, 0x118, 0xf, 0xf, false);
    return x;
}
DI uint32_t wave_scan(uint32_t x) {
    x = row_scan(x);
    x += __builtin_amdgcn_update_dpp(0u, x, 0x142, 0xa, 0xf, false);
    x += __builtin_amdgcn_update_dpp(0u, x, 0x143, 0xc, 0xf, false);
    return x;
}
template <int I> DI uint32_t row_bcast(uint32_t x) { return (uint32_t)__builtin_amdgcn_update_dpp(0, (int)x, 0x150 + I, 0xf, 0xf, false); }
DI uint32_t row_bcast_i(uint32_t x, int i) {
    switch (i) { case 0: return row_bcast<0>(x); case 1: return row_bcast<1>(x); case 2: return row_bcast<2>(x); case 3: return row_bcast<3>(x);
                 case 4: return row_bcast<4>(x); case 5: return row_bcast<5>(x); case 6: return row_bcast<6>(x); default: return row_bcast<7>(x); }
}
DI void dma16(uint32_t m0val, uint32_t voff, const __amdgpu_buffer_rsrc_t rs) {
    asm volatile("s_mov_b32 m0, %0\n\ts_nop 0\n\tbuffer_load_dwordx4 %1, %2, 0 offen lds" :: "s"(m0val), "v"(voff), "s"(rs) : "memory");
}
DI int lane_id() { return (int)__builtin_amdgcn_mbcnt_hi(~0u, __builtin_amdgcn_mbcnt_lo(~0u, 0u)); }
DI uint32_t mul24(uint32_t a, uint32_t b) { uint32_t r; asm("v_mul_u32_u24 %0, %1, %2" : "=v"(r) : "v"(a), "v"(b)); return r; }
DI uint32_t madi24(uint32_t a, int b, uint32_t c) { uint32_t r; asm("v_mad_i32_i24 %0, %1, %2, %3" : "=v"(r) : "v"(a), "v"(b), "v"(c)); return r; }

__global__ __launch_bounds__(1024, 4) void k_fused(const uint16_t* __restrict__ m, const int* __restrict__ idx_in, const int* __restrict__ idx_out,
                                                const uint16_t* __restrict__ dist, const uint16_t* __restrict__ angle, const uint16_t* __restrict__ centers,
                                                const uint16_t* __restrict__ freq, const uint16_t* __restrict__ w_rbf, const uint16_t* __restrict__ w_sbf,
                                                const uint16_t* __restrict__ w_down, const uint16_t* __restrict__ w_up, char* __restrict__ ws,
                                                uint16_t* __restrict__ out) {
    extern __shared__ __attribute__((aligned(16))) char smem[];
    const int tid = threadIdx.x, lane = tid & 63;
    const int v = __builtin_amdgcn_readfirstlane(tid >> 6);
    const int w = blockIdx.x;
    const unsigned long long tag = (*(const unsigned long long*)(ws + WS_EP) + 1ull) & TAGMASK;
    const int xc = w & 7, q = w >> 3;
    const int c = (xc < 2 ? xc * 32 : 64 + (xc - 2) * 31) + q;
    {
        if (tid < 256) *(u32a*)(smem + B_HIST + tid * 4) = 0;
        if (tid < 192) *(u32a*)(smem + tid * 4) = 0;
        const auto ro = __builtin_amdgcn_make_buffer_rsrc((void*)(idx_out + (size_t)w * CH), 0, CH * 4, 0x00020000);
        const auto ri = __builtin_amdgcn_make_buffer_rsrc((void*)(idx_in + (size_t)w * CH), 0, CH * 4, 0x00020000);
        const auto ra = __builtin_amdgcn_make_buffer_rsrc((void*)(angle + (size_t)w * CH), 0, CH * 2, 0x00020000);
        const u32x4 o0 = __builtin_amdgcn_raw_buffer_load_b128(ro, tid * 16, 0, AUX_NT), o1 = __builtin_amdgcn_raw_buffer_load_b128(ro, 16384 + tid * 16, 0, AUX_NT);
        const u32x2 o2 = __builtin_amdgcn_raw_buffer_load_b64(ro, 32768 + tid * 8, 0, AUX_NT);
        asm volatile("s_waitcnt lgkmcnt(0)\n\ts_barrier" ::: "memory");
        *(u32x4*)(smem + A_STAGE + v * WAVEB + lane * 16) = (u32x4){0u, 0u, 0u, 0u};
        *(u32x4*)(smem + A_STAGE + v * WAVEB + 1024 + lane * 16) = (u32x4){0u, 0u, 0u, 0u};
        const uint32_t o[10] = {o0[0], o0[1], o0[2], o0[3], o1[0], o1[1], o1[2], o1[3], o2[0], o2[1]};
        uint32_t b4[10], rk[10], el[10];
        const uint32_t k640 = 6710887u, one = 1u;
#pragma unroll
        for (int j = 0; j < 10; ++j) {
            const uint32_t b = mulhi24(o[j], k640);
            el[j] = madi24(b, -EB, o[j]);
            b4[j] = b * 4;
            asm volatile("ds_add_rtn_u32 %0, %1, %2 offset:%c3" : "=v"(rk[j]) : "v"(b4[j]), "v"(one), "i"(B_HIST) : "memory");
        }
        const u32x4 i0 = __builtin_amdgcn_raw_buffer_load_b128(ri, tid * 16, 0, AUX_NT); const u32x2 a0 = __builtin_amdgcn_raw_buffer_load_b64(ra, tid * 8, 0, AUX_NT);
        const u32x4 i1 = __builtin_amdgcn_raw_buffer_load_b128(ri, 16384 + tid * 16, 0, AUX_NT); const u32x2 a1 = __builtin_amdgcn_raw_buffer_load_b64(ra, 8192 + tid * 8, 0, AUX_NT);
        const u32x2 i2 = __builtin_amdgcn_raw_buffer_load_b64(ri, 32768 + tid * 8, 0, AUX_NT); const uint32_t a2 = __builtin_amdgcn_raw_buffer_load_b32(ra, 16384 + tid * 4, 0, AUX_NT);
        asm volatile("s_waitcnt lgkmcnt(0)\n\ts_barrier"
                     : "+v"(rk[0]), "+v"(rk[1]), "+v"(rk[2]), "+v"(rk[3]), "+v"(rk[4]), "+v"(rk[5]), "+v"(rk[6]), "+v"(rk[7]), "+v"(rk[8]), "+v"(rk[9]) :: "memory");
        const u32x4 c4 = *(const u32x4*)(smem + B_HIST + lane * 16);
        const uint32_t s4 = c4[0] + c4[1] + c4[2] + c4[3];
        const uint32_t ex = wave_scan(s4) - s4;
        const u32x4 bs4 = {ex, ex + c4[0], ex + c4[0] + c4[1], ex + c4[0] + c4[1] + c4[2]};
        *(u32x4*)(smem + B_BASE + lane * 16) = bs4;
        uint32_t bs[10];
#pragma unroll
        for (int j = 0; j < 10; ++j) asm volatile("ds_read_b32 %0, %1 offset:%c2" : "=v"(bs[j]) : "v"(b4[j]), "i"(B_BASE) : "memory");
        const uint32_t in[10] = {i0[0], i0[1], i0[2], i0[3], i1[0], i1[1], i1[2], i1[3], i2[0], i2[1]};
        const uint32_t an[10] = {a0[0], a0[0] >> 16, a0[1], a0[1] >> 16, a1[0], a1[0] >> 16, a1[1], a1[1] >> 16, a2, a2 >> 16};
        uint32_t w0[10];
#pragma unroll
        for (int j = 0; j < 10; ++j) w0[j] = ((el[j] + (in[j] >= HALF ? (uint32_t)EB : 0u)) << 20) + in[j];
        asm volatile("s_waitcnt lgkmcnt(0)"
                     : "+v"(bs[0]), "+v"(bs[1]), "+v"(bs[2]), "+v"(bs[3]), "+v"(bs[4]), "+v"(bs[5]), "+v"(bs[6]), "+v"(bs[7]), "+v"(bs[8]), "+v"(bs[9]) :: "memory");
        const uint32_t six = 6u;
#pragma unroll
        for (int j = 0; j < 10; ++j) {
            const uint32_t pos = bs[j] + rk[j], od = pos & 1u, t6 = mul24(pos, six);
            asm volatile("ds_write_b32 %0, %2 offset:%c4\n\tds_write_b16 %1, %3 offset:%c5"
                         :: "v"(madi24(od, -2, t6)), "v"(madi24(od, -4, t6)), "v"(w0[j]), "v"(an[j]), "i"(B_DENSE), "i"(B_DENSE + 8) : "memory");
        }
        if (v == 15) {
            const int x = lane & 15, g = lane >> 4;
            bf16x4 Asbf0, Asbf1, Arbf, Aup, Adn; f32x4 cen;
#pragma unroll
            for (int j = 0; j < 4; ++j) {
                const int r = (4 * g + j) & 7;
                Asbf0[j] = (short)w_sbf[(r * 2 + 0) * 16 + x];
                Asbf1[j] = (short)w_sbf[(r * 2 + 1) * 16 + x];
                Arbf[j] = (short)w_rbf[r * 16 + x];
                Aup[j] = (short)w_up[(4 * g + j) * 16 + x];
                Adn[j] = (short)w_down[(4 * g + j) * 16 + x];
                cen[j] = bf2f(centers[r]);
            }
            *(bf16x4*)(smem + A_WFR + 0 * 512 + lane * 8) = Asbf0;
            *(bf16x4*)(smem + A_WFR + 1 * 512 + lane * 8) = Asbf1;
            *(bf16x4*)(smem + A_WFR + 2 * 512 + lane * 8) = Arbf;
            *(bf16x4*)(smem + A_WFR + 3 * 512 + lane * 8) = Aup;
            *(bf16x4*)(smem + A_WFR + 4 * 512 + lane * 8) = Adn;
            *(f32x4*)(smem + A_WFR + 5 * 512 + lane * 16) = cen;
        }
        asm volatile("s_waitcnt lgkmcnt(0)\n\ts_barrier" ::: "memory");
        {
            const auto rs = __builtin_amdgcn_make_buffer_rsrc(ws + WS_REC + (size_t)w * CHS, 0, CHB, 0x00020000);
#pragma unroll
            for (int k = 0; k < 4; ++k) {
                const int off = (k * 1024 + tid) * 16;
                if (k < 3 || tid < 768) __builtin_amdgcn_raw_buffer_store_b128(*(const u32x4*)(smem + B_DENSE + off), rs, off, 0, AUX_SC1);
            }
        }
        asm volatile("s_waitcnt vmcnt(0)" ::: "memory");
        __syncthreads();
        if (v == 0) {
            const auto rs = __builtin_amdgcn_make_buffer_rsrc(ws + WS_TAB + (size_t)w * TABW * 8, 0, TABW * 8, 0x00020000);
            const uint32_t thi = (uint32_t)(tag >> 4), tlo = (uint32_t)(tag << 28);
            const u32x4 g0 = {tlo | (c4[0] << 14) | bs4[0], thi, tlo | (c4[1] << 14) | bs4[1], thi};
            const u32x4 g1 = {tlo | (c4[2] << 14) | bs4[2], thi, tlo | (c4[3] << 14) | bs4[3], thi};
            __builtin_amdgcn_raw_buffer_store_b128(g0, rs, lane * 32, 0, AUX_SC1);
            __builtin_amdgcn_raw_buffer_store_b128(g1, rs, lane * 32 + 16, 0, AUX_SC1);
        }
    }
    const uint32_t fq = *(const uint32_t*)freq;
    const float f0 = bf2f(fq & 0xFFFFu), f1 = __uint_as_float(fq & 0xFFFF0000u);
    const auto rsrc_m = __builtin_amdgcn_make_buffer_rsrc((void*)m, 0, E_ * 32, 0x00020000);
    const auto rsrc_r = __builtin_amdgcn_make_buffer_rsrc(ws + WS_REC, 0, NB * CHS, 0x00020000);

    uint32_t dr = 0;
    bool wave_pending = false;
    for (bool first = true;; first = false) {
        int lp = lane_id(); asm volatile("" : "+v"(lp));
        const uint32_t t2 = 2 * (lp & 31);
        uint32_t s_b0 = 0, s_rn = 0, s_odd = 0, pend = 0;
        const auto rs_seg = __builtin_amdgcn_make_buffer_rsrc(ws + WS_SEG + (size_t)(w * 16 + v) * 1024, 0, 1024, 0x00020000);
        if (!first) {
            const u32x4 sg = __builtin_amdgcn_raw_buffer_load_b128(rs_seg, lp * 16, 0, 0);
            s_b0 = sg[0]; s_rn = sg[1]; s_odd = sg[2]; pend = sg[3];
        }
        if (first) {
            const int wseg = v + 16 * (2 * (lp & 7) + (lp >> 5));
            const gu64* gp = (const gu64*)(ws + WS_TAB) + (size_t)min(wseg, NB - 1) * TABW + c;
            unsigned long long gr;
            for (unsigned spins = 0;; ++spins) {
                gr = __hip_atomic_load(gp, __ATOMIC_RELAXED, __HIP_MEMORY_SCOPE_AGENT);
                if (__builtin_amdgcn_ballot_w64((gr >> 28) != tag) == 0) break;
                if (spins > (1u << 18)) { gr = 0; break; }
                __builtin_amdgcn_s_sleep(2);
            }
            const uint32_t st = (uint32_t)gr & 0x3FFFu, n = ((uint32_t)gr >> 14) & 0x3FFFu;
            if (wseg < NB && n != 0) { s_b0 = wseg * CHS + (st >> 1) * 12; s_odd = st & 1u; s_rn = n + s_odd; }
        }
        const bool fresh = !wave_pending;
        const uint32_t left = s_rn > dr ? min(s_rn - dr, 127u) : 0u;
        const uint32_t wd = left == 0 ? 0u : (s_b0 + dr * 6) | (left << 24) | ((dr == 0 ? s_odd : 0u) << 31);
        const bool anylong = __builtin_amdgcn_ballot_w64(left > 64) != 0;
        const uint32_t tl = 64 + 2 * (lp & 7);
        u32x4 pl[2]; uint32_t dl[2];
#pragma unroll
        for (int i = 0; i < 2; ++i) { pl[i] = (u32x4){0u, 0u, 0u, 0u}; dl[i] = 0; asm volatile("" : "+v"(pl[i]), "+v"(dl[i])); }
        u32x4 pr[8];
#pragma unroll
        for (int i = 0; i < 8; ++i) {
            const uint32_t d = row_bcast_i(wd, i);
            uint32_t off = (d & 0xFFFFFFu) + t2 * 6;
            off = t2 < ((d >> 24) & 127u) ? off : 0x7FFFFFF0u;
            asm volatile("" : "+v"(off));
            pr[i] = __builtin_amdgcn_raw_buffer_load_b128(rsrc_r, off, 0, AUX_SC1);
        }
        if (__builtin_expect(anylong, 0)) {
#pragma unroll
            for (int i = 0; i < 2; ++i) {
                const int sg = 8 * i + (lp >> 3);
                dl[i] = (uint32_t)__builtin_amdgcn_ds_bpermute(((sg >> 1) + 32 * (sg & 1)) * 4, (int)wd);
                uint32_t off = (dl[i] & 0xFFFFFFu) + tl * 6;
                off = tl < ((dl[i] >> 24) & 127u) ? off : 0x7FFFFFF0u;
                asm volatile("" : "+v"(off));
                pl[i] = __builtin_amdgcn_raw_buffer_load_b128(rsrc_r, off, 0, AUX_SC1);
            }
        }
        uint32_t dv[3]; u32x2 mrow[3];
        {
            const int x = lp & 15, g = lp >> 4;
            const auto rs_d = __builtin_amdgcn_make_buffer_rsrc((void*)dist, 0, E_ * 2, 0x00020000);
#pragma unroll
            for (int s = 0; s < 3; ++s) {
                const int e = c * EB + (s < 2 ? v + 16 * s : 32 + (v & 7)) * 16 + x;
                dv[s] = __builtin_amdgcn_raw_buffer_load_b16(rs_d, e * 2, 0, 0);
                mrow[s] = __builtin_amdgcn_raw_buffer_load_b64(rsrc_m, e * 32 + g * 8, 0, 0);
            }
        }
        uint32_t newpend = 0;
        const uint32_t klw = LW, kla = LA;
        auto place = [&](const u32x4& pa, const u32x4& pb, const uint32_t da, const uint32_t db, const uint32_t tt0, const bool head, const int sb) {
            uint64_t mk[4]; uint32_t ha[4], lw[4], la[4], av[4];
            asm volatile("" :: "v"(pa), "v"(pb));
#pragma unroll
            for (int t = 0; t < 4; ++t) {
                const u32x4& p = t < 2 ? pa : pb;
                const uint32_t d = t < 2 ? da : db;
                bool ok = tt0 + (t & 1) < ((d >> 24) & 127u);
                if ((t & 1) == 0 && head) ok = ok && !(t2 == 0 && (int)d < 0);
                mk[t] = __builtin_amdgcn_ballot_w64(ok);
                const uint32_t key = p[t & 1] >> 24;
                ha[t] = key * 4; lw[t] = mul24(key, klw); la[t] = mul24(key, kla);
                av[t] = (t & 1) ? p[2] >> 16 : p[2];
            }
            if (__builtin_expect(!fresh, 0)) {
#pragma unroll
                for (int t = 0; t < 4; ++t) mk[t] &= __builtin_amdgcn_ballot_w64(((pend >> (sb + t)) & 1u) != 0);
            }
            uint32_t rank[4] = {0u, 0u, 0u, 0u};
            const uint32_t one = 1u;
            asm volatile("s_mov_b64 exec, %4\n\tds_add_rtn_u32 %0, %8, %12\n\t"
                         "s_mov_b64 exec, %5\n\tds_add_rtn_u32 %1, %9, %12\n\t"
                         "s_mov_b64 exec, %6\n\tds_add_rtn_u32 %2, %10, %12\n\t"
                         "s_mov_b64 exec, %7\n\tds_add_rtn_u32 %3, %11, %12\n\t"
                         "s_mov_b64 exec, -1\n\ts_waitcnt lgkmcnt(0)"
                         : "+v"(rank[0]), "+v"(rank[1]), "+v"(rank[2]), "+v"(rank[3])
                         : "s"(mk[0]), "s"(mk[1]), "s"(mk[2]), "s"(mk[3]), "v"(ha[0]), "v"(ha[1]), "v"(ha[2]), "v"(ha[3]), "v"(one)
                         : "memory");
#pragma unroll
            for (int t = 0; t < 4; ++t) { const uint32_t rc = min(rank[t], (uint32_t)CAPT); lw[t] += rc * 4; la[t] += rc * 2; }
            asm volatile("s_mov_b64 exec, %0\n\tds_write_b32 %4, %12 offset:%c20\n\tds_write_b16 %8, %16 offset:%c21\n\t"
                         "s_mov_b64 exec, %1\n\tds_write_b32 %5, %13 offset:%c20\n\tds_write_b16 %9, %17 offset:%c21\n\t"
                         "s_mov_b64 exec, %2\n\tds_write_b32 %6, %14 offset:%c20\n\tds_write_b16 %10, %18 offset:%c21\n\t"
                         "s_mov_b64 exec, %3\n\tds_write_b32 %7, %15 offset:%c20\n\tds_write_b16 %11, %19 offset:%c21\n\t"
                         "s_mov_b64 exec, -1"
                         :: "s"(mk[0]), "s"(mk[1]), "s"(mk[2]), "s"(mk[3]), "v"(lw[0]), "v"(lw[1]), "v"(lw[2]), "v"(lw[3]),
                            "v"(la[0]), "v"(la[1]), "v"(la[2]), "v"(la[3]), "v"(pa[0]), "v"(pa[1]), "v"(pb[0]), "v"(pb[1]),
                            "v"(av[0]), "v"(av[1]), "v"(av[2]), "v"(av[3]), "i"(A_LISTW), "i"(A_LISTA)
                         : "memory");
            if (__builtin_expect(__builtin_amdgcn_ballot_w64(max(max(rank[0], rank[1]), max(rank[2], rank[3])) >= CAPT) != 0, 0)) {
#pragma unroll
                for (int t = 0; t < 4; ++t) newpend |= (rank[t] >= CAPT ? 1u : 0u) << (sb + t);
            }
        };
#pragma unroll
        for (int i0 = 0; i0 < 8; i0 += 2) place(pr[i0], pr[i0 + 1], row_bcast_i(wd, i0), row_bcast_i(wd, i0 + 1), t2, true, 2 * i0);
        if (anylong) place(pl[0], pl[1], dl[0], dl[1], tl, false, 16);
        pend = newpend;
        wave_pending = __builtin_amdgcn_ballot_w64(pend != 0) != 0;
        if (!wave_pending) dr += 80;
        const bool wave_more = wave_pending || __builtin_amdgcn_ballot_w64(s_rn > dr) != 0;
        if (wave_more && lp == 0) *(u32a*)(smem + A_FLAG) = 1;
        __syncthreads();
        const bool more = *(const u32a*)(smem + A_FLAG) != 0;
        if (more) __builtin_amdgcn_raw_buffer_store_b128((u32x4){s_b0, s_rn, s_odd, pend}, rs_seg, lp * 16, 0, 0);
        {
            int ln = lane_id(); asm volatile("" : "+v"(ln));
            const int xx = ln & 15, gg = ln >> 4;
            const int rho = ln >> 1;
            const int kpos = 8 * ((rho >> 2) & 3) + 4 * (rho >> 4) + (rho & 3);
            const uint32_t lsel = ln & 1;
            const uint32_t sc_const = (uint32_t)(((kpos >> 3) * 16) | ((kpos & 7) * 2)) | (lsel * (1024 + 64));
            const uint32_t rd0 = (uint32_t)(xx * 64 + ((gg ^ ((0 - (xx >> 2)) & 3)) * 16));
            const uint32_t rd1 = (uint32_t)(1024 + (xx ^ 1) * 64 + ((gg ^ ((0 - (xx >> 2)) & 3)) * 16));
            const uint32_t half16 = lsel * 16, posw = kpos * 4, posa = kpos * 2;
            char* stage = smem + A_STAGE + v * WAVEB;
            const uint32_t ring = A_STAGE + v * WAVEB + 2048;
            const uint32_t trb = ring + (4 * gg + (xx >> 2)) * 32 + (xx & 3) * 8;
            const float fl = lsel ? f1 : f0;
            const int hi = v >> 3;
            const int jl = min(ln, 5);
            const int tl_ = jl == 0 || jl == 4 ? v : (jl == 1 || jl == 5 ? v + 16 : 32 + (v & 7));
            const int keyl = tl_ + (jl >= 3 ? NTILE : 0);
            const int cntl = min((int)*(const u32a*)(smem + A_HIST + keyl * 4), CAPT);
            const int nall = (cntl + 31) >> 5;
            const bool shared_l = jl == 2 || jl == 3;
            const int kf = shared_l && hi ? (nall + 1) >> 1 : 0;
            const int nsl = ln < 6 ? (shared_l ? (hi ? nall - kf : (nall + 1) >> 1) : nall) : 0;
            const int cnt0 = __builtin_amdgcn_readlane(cntl, 0);
            if (cnt0 > 0) {
                const uint32_t wf = *(const u32a*)(smem + A_LISTW + v * LW + posw);
                dma16(ring, (kpos < cnt0 ? wf & IDXMASK : IDXMASK) * 32 + half16, rsrc_m);
            }
            int nst[6]; int cum = 0;
            uint32_t stepw = A_NULL, stepa = A_NULL, steprem = 0;
#pragma unroll
            for (int j = 0; j < 6; ++j) {
                const int ns = __builtin_amdgcn_readlane(nsl, j), cnt = __builtin_amdgcn_readlane(cntl, j), key = __builtin_amdgcn_readlane(keyl, j), k0 = __builtin_amdgcn_readlane(kf, j);
                if ((uint32_t)(ln - cum) < (uint32_t)ns) {
                    const int k = k0 + ln - cum;
                    stepw = A_LISTW + key * LW + k * 128; stepa = A_LISTA + key * LA + k * 64; steprem = cnt - k * 32;
                }
                nst[j] = ns; cum += ns;
            }
#define LIVE(t) ((uint32_t)kpos < (uint32_t)__builtin_amdgcn_readlane(steprem, (t)))
#define LDW(t) (*(const u32a*)(smem + __builtin_amdgcn_readlane(stepw, (t)) + posw))
#define LDA(t) ((uint32_t)*(const u16a*)(smem + __builtin_amdgcn_readlane(stepa, (t)) + posa))
#define SELV(ang) cvt_pk(cos_rad(rbf16(fl * bf2f(ang))), 0.f)
#define SELA(w) ((((w) >> 14) & 0x3C0u) | ((0u - (((w) >> 18) & 0x30u)) & 0x30u))
            uint32_t sa;
            uint32_t w1, a1;
            {
                const uint32_t w0 = LDW(0), a0 = LDA(0);
                w1 = LDW(1); a1 = LDA(1);
                const bool lv = LIVE(0);
                if (cnt0 <= 0) dma16(ring, (lv ? w0 & IDXMASK : IDXMASK) * 32 + half16, rsrc_m);
                sa = (lv ? SELA(w0) : 0u) ^ sc_const;
                *(u16a*)(stage + sa) = (uint16_t)SELV(lv ? a0 : 0u);
            }
            f32x4 acc0[3], acc1[3];
            const auto rs_park = __builtin_amdgcn_make_buffer_rsrc(ws + WS_ACC + (size_t)(w * 16 + v) * 24 * 256, 0, 24 * 256, 0x00020000);
#pragma unroll
            for (int s = 0; s < 3; ++s) {
#pragma unroll
                for (int j = 0; j < 4; ++j) { acc0[s][j] = 0.f; acc1[s][j] = 0.f; }
            }
            if (!first) {
#pragma unroll
                for (int s = 0; s < 3; ++s) {
#pragma unroll
                    for (int j = 0; j < 4; ++j) {
                        acc0[s][j] = __uint_as_float(__builtin_amdgcn_raw_buffer_load_b32(rs_park, ln * 4, (s * 8 + j) * 256, 0));
                        acc1[s][j] = __uint_as_float(__builtin_amdgcn_raw_buffer_load_b32(rs_park, ln * 4, (s * 8 + 4 + j) * 256, 0));
                    }
                }
                asm volatile("s_waitcnt vmcnt(0)" ::: "memory");
            }
            auto finish = [&](const int tile, const f32x4& u0f, const f32x4& u1f, uint32_t dvs, u32x2 mrs) {
                asm volatile("" : "+v"(dvs), "+v"(mrs));
                const float d = bf2f(dvs);
                const bf16x4 Asbf0 = *(const bf16x4*)(smem + A_WFR + 0 * 512 + ln * 8), Asbf1 = *(const bf16x4*)(smem + A_WFR + 1 * 512 + ln * 8);
                const bf16x4 Arbf = *(const bf16x4*)(smem + A_WFR + 2 * 512 + ln * 8), Aup = *(const bf16x4*)(smem + A_WFR + 3 * 512 + ln * 8);
                const bf16x4 Adn = *(const bf16x4*)(smem + A_WFR + 4 * 512 + ln * 8);
                const f32x4 cen = *(const f32x4*)(smem + A_WFR + 5 * 512 + ln * 16);
                float rb[4];
#pragma unroll
                for (int t = 0; t < 4; ++t) {
                    const float df = rbf16(d - cen[t]);
                    const float sq = rbf16(df * df);
                    const float ar = rbf16(-10.f * sq);
                    rb[t] = gg < 2 ? __expf(ar) : 0.f;
                }
                const u32x2 rbp = {cvt_pk(rb[0], rb[1]), cvt_pk(rb[2], rb[3])};
                const bf16x4 Brbf = __builtin_bit_cast(bf16x4, rbp);
                const f32x4 z = {0.f, 0.f, 0.f, 0.f};
                const f32x4 A0 = __builtin_amdgcn_mfma_f32_16x16x16bf16_1k(Asbf0, Brbf, z, 0, 0, 0);
                const f32x4 A1 = __builtin_amdgcn_mfma_f32_16x16x16bf16_1k(Asbf1, Brbf, z, 0, 0, 0);
                const f32x4 G = __builtin_amdgcn_mfma_f32_16x16x16bf16_1k(Arbf, Brbf, z, 0, 0, 0);
                const u32x2 u0 = {cvt_pk(u0f[0], u0f[1]), cvt_pk(u0f[2], u0f[3])};
                const u32x2 u1 = {cvt_pk(u1f[0], u1f[1]), cvt_pk(u1f[2], u1f[3])};
                const f32x4 S0 = __builtin_amdgcn_mfma_f32_16x16x16bf16_1k(Adn, __builtin_bit_cast(bf16x4, u0), z, 0, 0, 0);
                const f32x4 S1 = __builtin_amdgcn_mfma_f32_16x16x16bf16_1k(Adn, __builtin_bit_cast(bf16x4, u1), z, 0, 0, 0);
                float ag[4];
#pragma unroll
                for (int t = 0; t < 4; ++t) ag[t] = A0[t] * S0[t] + A1[t] * S1[t];
                const u32x2 agp = {cvt_pk(ag[0], ag[1]), cvt_pk(ag[2], ag[3])};
                const f32x4 Y = __builtin_amdgcn_mfma_f32_16x16x16bf16_1k(Aup, __builtin_bit_cast(bf16x4, agp), z, 0, 0, 0);
                const float o0 = bf2f(mrs[0] & 0xFFFFu) + Y[0] * G[0], o1 = __uint_as_float(mrs[0] & 0xFFFF0000u) + Y[1] * G[1];
                const float o2 = bf2f(mrs[1] & 0xFFFFu) + Y[2] * G[2], o3 = __uint_as_float(mrs[1] & 0xFFFF0000u) + Y[3] * G[3];
                const u32x2 ov = {cvt_pk(o0, o1), cvt_pk(o2, o3)};
                const auto rs_out = __builtin_amdgcn_make_buffer_rsrc(out + (size_t)(c * EB + tile * 16) * 16, 0, 512, 0x00020000);
                __builtin_amdgcn_raw_buffer_store_b64(ov, rs_out, xx * 32 + gg * 8, 0, AUX_NT);
            };
            const int vh = (v & 7) + 8;
            auto share_addr = [&](const int r) -> uint32_t {
                return r < 3 ? A_LISTW + vh * LW + r * 256 : r < 6 ? A_LISTW + (vh + 16) * LW + (r - 3) * 256 : A_LISTA + (r == 6 ? vh : vh + 16) * LA;
            };
            int i = 0; uint32_t slot = 0;
#pragma unroll
            for (int j = 0; j < 6; ++j) {
                const int s = j == 0 || j == 4 ? 0 : (j == 1 || j == 5 ? 1 : 2);
                if (j == 3) {
                    asm volatile("" : "+v"(dv[0]), "+v"(dv[1]), "+v"(dv[2]), "+v"(mrow[0]), "+v"(mrow[1]), "+v"(mrow[2]));
                }
                for (int k = 0; k < nst[j]; ++k) {
                    const bool lv = LIVE(i + 1);
                    dma16(ring + (slot ^ 1024), (lv ? w1 & IDXMASK : IDXMASK) * 32 + half16, rsrc_m);
                    asm volatile("s_waitcnt vmcnt(1)" ::: "memory");
                    const s16x4 alo = __builtin_amdgcn_ds_read_tr16_b64_v4i16((lds_s16x4_p)(uintptr_t)(trb + slot));
                    const s16x4 ahi = __builtin_amdgcn_ds_read_tr16_b64_v4i16((lds_s16x4_p)(uintptr_t)(trb + slot + 512));
                    const u32x4 s0 = *(const u32x4*)(stage + rd0);
                    const u32x4 s1 = *(const u32x4*)(stage + rd1);
                    *(u16a*)(stage + sa) = 0;
                    sa = (lv ? SELA(w1) : 0u) ^ sc_const;
                    *(u16a*)(stage + sa) = (uint16_t)SELV(lv ? a1 : 0u);
                    ++i;
                    w1 = LDW(i + 1); a1 = LDA(i + 1);
                    const bf16x8 a = __builtin_shufflevector(alo, ahi, 0, 1, 2, 3, 4, 5, 6, 7);
                    acc0[s] = __builtin_amdgcn_mfma_f32_16x16x32_bf16(a, __builtin_bit_cast(bf16x8, s0), acc0[s], 0, 0, 0);
                    acc1[s] = __builtin_amdgcn_mfma_f32_16x16x32_bf16(a, __builtin_bit_cast(bf16x8, s1), acc1[s], 0, 0, 0);
                    slot ^= 1024;
                }
                if (!more) {
                    if (j == 3 && hi) {
#pragma unroll
                        for (int r = 0; r < 8; ++r) *(u32a*)(smem + share_addr(r) + ln * 4) = __float_as_uint(r < 4 ? acc0[2][r] : acc1[2][r - 4]);
                        asm volatile("s_waitcnt lgkmcnt(0)" ::: "memory");
                        if (ln == 0) *(lds_vu32*)(uintptr_t)(A_FLAG + 4 + (v & 7) * 4) = 1;
                    }
                    if (j >= 4) finish(v + 16 * (j - 4), acc0[j - 4], acc1[j - 4], dv[j - 4], mrow[j - 4]);
                    if (j == 4 && !hi) {
                        while (__builtin_amdgcn_readfirstlane(*(lds_vu32*)(uintptr_t)(A_FLAG + 4 + v * 4)) == 0) __builtin_amdgcn_s_sleep(1);
#pragma unroll
                        for (int r = 0; r < 8; ++r) {
                            const float p = __uint_as_float(*(const u32a*)(smem + share_addr(r) + ln * 4));
                            if (r < 4) acc0[2][r] += p; else acc1[2][r - 4] += p;
                        }
                        finish(32 + v, acc0[2], acc1[2], dv[2], mrow[2]);
                    }
                }
            }
            *(u16a*)(stage + sa) = 0;
            if (more) {
#pragma unroll
                for (int s = 0; s < 3; ++s) {
#pragma unroll
                    for (int j = 0; j < 4; ++j) {
                        __builtin_amdgcn_raw_buffer_store_b32(__float_as_uint(acc0[s][j]), rs_park, ln * 4, (s * 8 + j) * 256, 0);
                        __builtin_amdgcn_raw_buffer_store_b32(__float_as_uint(acc1[s][j]), rs_park, ln * 4, (s * 8 + 4 + j) * 256, 0);
                    }
                }
            }
            asm volatile("s_waitcnt vmcnt(0)" ::: "memory");
        }
        if (!more) break;
        __syncthreads();
        if (v < 2) { int t = lane_id(); asm volatile("" : "+v"(t)); t += v * 64; if (t < 97) *(u32a*)(smem + t * 4) = 0; }
        __syncthreads();
    }
    if (w == 0 && v == 0 && lane_id() == 0) *(unsigned long long*)(ws + WS_EP) = tag;
}

extern "C" void kernel_launch(void* const* d_in, const int* in_sizes, int n_in,
                              void* d_out, int out_size, void* d_ws, size_t ws_size,
                              hipStream_t stream) {
    (void)hipFuncSetAttribute((const void*)k_fused, hipFuncAttributeMaxDynamicSharedMemorySize, LDSF);
    k_fused<<<NB, 1024, LDSF, stream>>>((const uint16_t*)d_in[0], (const int*)d_in[1], (const int*)d_in[2], (const uint16_t*)d_in[3], (const uint16_t*)d_in[4],
                                        (const uint16_t*)d_in[5], (const uint16_t*)d_in[6], (const uint16_t*)d_in[7], (const uint16_t*)d_in[8],
                                        (const uint16_t*)d_in[9], (const uint16_t*)d_in[10], (char*)d_ws, (uint16_t*)d_out);
}
```
